```python
import torch

def _softmax(x, axis=-1):
    return torch.softmax(x, dim=axis)
def _gelu(x):
    return torch.nn.functional.gelu(x, approximate="tanh")
def _silu(x):
    return torch.nn.functional.silu(x)
def _sigmoid(x):
    return torch.sigmoid(x)
def _layernorm(x, axis=-1, eps=1e-5):
    m = x.mean(dim=axis, keepdim=True)
    v = x.var(dim=axis, keepdim=True, unbiased=False)
    return (x - m) / torch.sqrt(v + eps)
def _scatter_add(tgt, idx, src, axis=0):
    return tgt.clone().index_add_(axis, idx, src)
def _segment_sum(src, seg, n):
    out = torch.zeros((n,) + src.shape[1:], dtype=src.dtype)
    return out.index_add_(0, seg, src)
def _pad(x, pw):
    flat = tuple(v for pair in reversed(pw) for v in pair)
    return torch.nn.functional.pad(x, flat)

kernel_name = 'standalone__instancenorm__prompt'

def setup_inputs():

    _w = (lambda a: (lambda t: t.to(torch.bfloat16) if t.is_floating_point() else t)(torch.as_tensor(a)))
    B, C, H, W = 2, 640, 2048, 32
    return {
        "x": _w(abs((lambda *s: torch.randn(*s, dtype=torch.bfloat16) * 0.05)(B, C, H, W))),
        "g": _w((lambda *s: torch.randn(*s, dtype=torch.bfloat16) * 0.05)(C) + 1.0),
        "b": _w((lambda *s: torch.randn(*s, dtype=torch.bfloat16) * 0.4)(C)),
    }

def reference(x, g, b):
    mu = x.mean(axis=(-2, -1), keepdims=True)
    var = ((x - mu) ** 2).mean(axis=(-2, -1), keepdims=True)
    xhat = (x - mu) / torch.sqrt(var + 1e-5)
    return xhat * g.reshape(1, -1, 1, 1) + b.reshape(1, -1, 1, 1)
```

```cpp
#include <hip/hip_runtime.h>
#include <hip/hip_bf16.h>
#include <stdint.h>

typedef unsigned int u32x4 __attribute__((ext_vector_type(4)));
typedef float f32x2 __attribute__((ext_vector_type(2)));
typedef __bf16 bf16x2 __attribute__((ext_vector_type(2)));
typedef unsigned long long u64;
typedef __attribute__((address_space(1))) u64 gu64;

constexpr int HW = 2048 * 32;
constexpr unsigned PLANE_BYTES = HW * 2;
constexpr unsigned CHUNK_BYTES = 8192;
constexpr int CHUNK_ELEMS = 4096;
constexpr int NCHUNK = 16;
constexpr int PLANES_PER_PAGE = 16;
constexpr size_t PAGE_BYTES = (size_t)PLANES_PER_PAGE * PLANE_BYTES;
constexpr unsigned NPAGES = 80;
constexpr int UPW = 2;
constexpr unsigned BOX_BYTES = 384;
constexpr unsigned ADVERT = UPW * 8;
constexpr unsigned HINT_BYTES = 128;
constexpr int LDAUX = 2;
constexpr int STAUX = 16;
constexpr int SC1 = 16;
constexpr u64 TIMEOUT_TICKS = 5000;
constexpr unsigned HINT_WAIT = 2500;
constexpr unsigned HOLD = 80, AFTER = 10;
constexpr unsigned QUIET = 190;
constexpr unsigned EAGER = 16;
constexpr int NSAMP = 4;
constexpr unsigned CLEAR = 128, MARGIN = 64;

__device__ __forceinline__ float bf_lo(unsigned v) { return __builtin_bit_cast(float, v << 16); }
__device__ __forceinline__ float bf_hi(unsigned v) { return __builtin_bit_cast(float, v & 0xffff0000u); }
__device__ __forceinline__ unsigned pack_bf16(f32x2 f) {
    return __builtin_bit_cast(unsigned, __builtin_convertvector(f, bf16x2));
}
__device__ __forceinline__ unsigned xcc_id() {
    unsigned v;
    asm volatile("s_getreg_b32 %0, hwreg(HW_REG_XCC_ID)" : "=s"(v));
    return v & 0xf;
}
template <int CTRL> __device__ __forceinline__ float dpp_add(float v) {
    int o = __builtin_amdgcn_update_dpp(0, __builtin_bit_cast(int, v), CTRL, 0xf, 0xf, true);
    return v + __builtin_bit_cast(float, o);
}
__device__ __forceinline__ float row_sum(float v) {
    v = dpp_add<0xB1>(v);
    v = dpp_add<0x4E>(v);
    v = dpp_add<0x141>(v);
    v = dpp_add<0x140>(v);
    return v;
}
__device__ __forceinline__ float rl(float v, int l) {
    return __builtin_bit_cast(float, __builtin_amdgcn_readlane(__builtin_bit_cast(int, v), l));
}
__device__ __forceinline__ float wave_sum(float v) {
    v = row_sum(v);
    return (rl(v, 0) + rl(v, 16)) + (rl(v, 32) + rl(v, 48));
}
__device__ __forceinline__ f32x2 acc_sum(f32x2 s, u32x4 v) {
#pragma unroll
    for (int j = 0; j < 4; ++j) s += f32x2{bf_lo(v[j]), bf_hi(v[j])};
    return s;
}
__device__ __forceinline__ f32x2 acc_sq(f32x2 q, u32x4 v, f32x2 m) {
#pragma unroll
    for (int j = 0; j < 4; ++j) {
        f32x2 d = f32x2{bf_lo(v[j]), bf_hi(v[j])} - m;
        q = __builtin_elementwise_fma(d, d, q);
    }
    return q;
}
__device__ __forceinline__ u32x4 normalize(u32x4 v, f32x2 m, f32x2 sc, f32x2 sh) {
    u32x4 o;
#pragma unroll
    for (int j = 0; j < 4; ++j) {
        f32x2 d = f32x2{bf_lo(v[j]), bf_hi(v[j])} - m;
        o[j] = pack_bf16(__builtin_elementwise_fma(d, sc, sh));
    }
    return o;
}
__device__ __forceinline__ unsigned hash32(unsigned v) {
    v *= 2654435761u; v ^= v >> 15; v *= 2246822519u; v ^= v >> 13;
    return v;
}
__device__ __forceinline__ unsigned umin_(unsigned a, unsigned b) { return a < b ? a : b; }
__device__ __forceinline__ auto chunk_rsrc(const char* p) {
    return __builtin_amdgcn_make_buffer_rsrc((void*)p, 0, CHUNK_BYTES, 0x00020000);
}
__device__ __forceinline__ unsigned probe_par(unsigned i) { return (i ^ (i >> 1)) & 1; }
__device__ __forceinline__ unsigned probe_off(unsigned pg, unsigned wave, unsigned i, unsigned salt) {
    const unsigned h = hash32(((pg * 4 + wave) * 16 + i) ^ salt * 0x9e3779b1u);
    return ((h >> 8) & 127) * (2 * CHUNK_BYTES) + probe_par(i) * CHUNK_BYTES + (h & 63) * 128;
}
__device__ __forceinline__ unsigned chunk_of(unsigned code, unsigned w) {
    return (code & 2) ? 2 * w + (code & 1) : (w >> 1) * 4 + (w & 1) + 2 * (code & 1);
}
__device__ __forceinline__ unsigned set_of(unsigned code) {
    return (code & 2) ? 0x5555u << (code & 1) : 0x3333u << (2 * (code & 1));
}
__device__ __forceinline__ unsigned read_hint(gu64* hint, u64 t_start, unsigned role) {
    const u64 lo = t_start - 2000;
    for (;;) {
        const u64 h = __hip_atomic_load(hint, __ATOMIC_RELAXED, __HIP_MEMORY_SCOPE_AGENT);
        const unsigned hl = __builtin_amdgcn_readfirstlane((unsigned)h);
        const unsigned hh = __builtin_amdgcn_readfirstlane((unsigned)(h >> 32));
        const u64 now = __builtin_amdgcn_s_memrealtime();
        const u64 age = ((((u64)hh << 32 | hl) >> 8) - lo) & 0xffffffffffffffull;
        if (age <= now - t_start + 2016) return (hl & 1u) ? 2u | (((hl >> 1) ^ (xcc_id() >> 2)) & 1u) : role;
        if (now - t_start > HINT_WAIT) return role;
        __builtin_amdgcn_s_sleep(2);
    }
}

__global__ __launch_bounds__(256, 5) void instnorm_pair_kernel(const char* __restrict__ x, const uint16_t* __restrict__ g,
                                                               const uint16_t* __restrict__ b, char* __restrict__ y,
                                                               char* ws, int C) {
    __shared__ f32x2 stat[UPW][NCHUNK];
    __shared__ unsigned sh_missing[UPW];
    __shared__ unsigned sh_code[UPW];
    __shared__ unsigned sh_lat[8];
    const u64 t_start = __builtin_amdgcn_s_memrealtime();
    const unsigned bid = blockIdx.x;
    const unsigned grp = bid & 3, role = (bid >> 2) & 1, q = bid >> 3;
    const unsigned wave = __builtin_amdgcn_readfirstlane(threadIdx.x >> 6), lane = threadIdx.x & 63;
    const unsigned sp = q >> 3, s5 = sp / 5, r5 = sp - s5 * 5;
    const unsigned page = (r5 == 4 ? NPAGES / 5 + s5 : s5 * 4 + r5) * 4 + grp;
    const unsigned inst0 = page * PLANES_PER_PAGE + (q & 7) * UPW;
    char* boxes = ws + NPAGES * HINT_BYTES;
    const auto rin = __builtin_amdgcn_make_buffer_rsrc(boxes + (size_t)bid * BOX_BYTES, 0, BOX_BYTES, 0x00020000);
    char* outbox = boxes + (size_t)(bid ^ 4) * BOX_BYTES;
    const auto rout = __builtin_amdgcn_make_buffer_rsrc(outbox, 0, BOX_BYTES, 0x00020000);
    gu64* hint = (gu64*)(ws + (size_t)page * HINT_BYTES);
    const bool eager = q < EAGER;

    unsigned code = role;
    {
        const unsigned xcc = xcc_id();
        const bool quiet = xcc == 0 || xcc == 2;
        if (quiet && q < NPAGES / 2) {
            const unsigned pg = 2 * q + (xcc >> 1), salt = (unsigned)(t_start >> 11);
            const char* buf = wave < 2 ? x : y;
            {
                const char* t = lane < NSAMP ? buf + (size_t)(pg ^ 1) * PAGE_BYTES + probe_off(pg ^ 1, wave, lane, salt)
                                             : buf + (size_t)pg * PAGE_BYTES + (PAGE_BYTES - 128);
                unsigned w0;
                if (lane <= NSAMP)
                    asm volatile("global_load_dword %0, %1, off\n\ts_waitcnt vmcnt(0)" : "=v"(w0) : "v"(t) : "memory");
            }
            const u64 t1 = __builtin_amdgcn_s_memrealtime() + AFTER;
            while (__builtin_amdgcn_s_memrealtime() < t1 || __builtin_amdgcn_s_memrealtime() - t_start < HOLD)
                __builtin_amdgcn_s_sleep(1);
            unsigned lat[2] = {4095u, 4095u};
#pragma unroll
            for (int i = 0; i < NSAMP; ++i) {
                if (i == 2 && lat[0] < 4095u && lat[1] < 4095u && (lat[0] + CLEAR <= lat[1] || lat[1] + CLEAR <= lat[0]))
                    break;
                const char* p = buf + (size_t)pg * PAGE_BYTES + probe_off(pg, wave, i, salt);
                u64 c0, c1;
                unsigned v;
                if (lane == 0)
                    asm volatile("s_memtime %0\n\ts_waitcnt lgkmcnt(0)\n\t"
                                 "global_load_dword %2, %3, off\n\ts_waitcnt vmcnt(0)\n\t"
                                 "s_memtime %1\n\ts_waitcnt lgkmcnt(0)"
                                 : "=&s"(c0), "=&s"(c1), "=&v"(v) : "v"(p) : "memory");
                unsigned d = __builtin_amdgcn_readfirstlane((unsigned)(c1 - c0));
                d = d < 336u ? 4095u : d;
                lat[probe_par(i)] = umin_(lat[probe_par(i)], d);
            }
            if (lane == 0) {
                sh_lat[wave * 2] = lat[0];
                sh_lat[wave * 2 + 1] = lat[1];
            }
            __syncthreads();
            if (threadIdx.x == 0) {
                const unsigned xe = umin_(sh_lat[0], sh_lat[2]), xo = umin_(sh_lat[1], sh_lat[3]);
                const unsigned ye = umin_(sh_lat[4], sh_lat[6]), yo = umin_(sh_lat[5], sh_lat[7]);
                const unsigned ev = xe + MARGIN <= xo && ye + MARGIN <= yo, od = xo + MARGIN <= xe && yo + MARGIN <= ye;
                __hip_atomic_store((gu64*)(ws + (size_t)pg * HINT_BYTES),
                                   (u64)((ev | od) | od << 1) | __builtin_amdgcn_s_memrealtime() << 8, __ATOMIC_RELAXED,
                                   __HIP_MEMORY_SCOPE_AGENT);
            }
        }
        if (quiet) {
            while (__builtin_amdgcn_s_memrealtime() - t_start < (q < 48 ? QUIET + 40 : QUIET)) __builtin_amdgcn_s_sleep(2);
        } else if (!eager) {
            const char* t = (lane == 0 ? x : y) + (size_t)inst0 * PLANE_BYTES + wave * 4096;
            unsigned w0;
            if (lane < 2) asm volatile("global_load_dword %0, %1, off\n\ts_waitcnt vmcnt(0)" : "=v"(w0) : "v"(t) : "memory");
        }
        if (!eager) {
            if (wave == 0) {
                const unsigned c = read_hint(hint, t_start, role);
                if (lane == 0) sh_code[0] = c;
            }
            __syncthreads();
            code = __builtin_amdgcn_readfirstlane(sh_code[0]);
        }
    }

    unsigned mytag = 0, ptag = 0;
    bool verified = false;
#pragma unroll 1
    for (unsigned u = 0; u < UPW; ++u) {
        const unsigned inst = inst0 + u;
        const size_t pbase = (size_t)inst * PLANE_BYTES;
        unsigned voff = lane * 16u;
        asm volatile("" : "+v"(voff));
        const bool first = u == 0 && wave == 0;
        u32x4 old = {0, 0, 0, 0};
        if (first) old = __builtin_amdgcn_raw_buffer_load_b128(rin, voff, 0, SC1);
        u32x4 v[2][8];
#pragma unroll
        for (int c = 0; c < 2; ++c) {
            const auto rx = chunk_rsrc(x + pbase + chunk_of(code, wave * 2 + c) * CHUNK_BYTES);
#pragma unroll
            for (int k = 0; k < 8; ++k) v[c][k] = __builtin_amdgcn_raw_buffer_load_b128(rx, voff + k * 1024u, 0, LDAUX);
        }
        const unsigned ch = inst % (unsigned)C;
        const float gamma = __builtin_bit_cast(float, (unsigned)g[ch] << 16);
        const float beta = __builtin_bit_cast(float, (unsigned)b[ch] << 16);
        if (first) {
            const unsigned t0 = old[1] & 0x3fu, t1 = old[3] & 0x3fu;
            mytag = 1;
            while (__ballot(t0 == mytag || t1 == mytag)) ++mytag;
            if (lane == 0)
                __hip_atomic_store((gu64*)(outbox + ADVERT * 16), (u64)(mytag << 18) << 32, __ATOMIC_RELAXED,
                                   __HIP_MEMORY_SCOPE_AGENT);
        }
#pragma unroll
        for (int c = 0; c < 2; ++c) {
            f32x2 su = {0.f, 0.f};
#pragma unroll
            for (int k = 0; k < 8; ++k) su = acc_sum(su, v[c][k]);
            const float cm = wave_sum(su.x + su.y) * (1.0f / CHUNK_ELEMS);
#pragma unroll
            for (int k = 0; k < 8; ++k) asm volatile("" : "+v"(v[c][k]));
            f32x2 sq = {0.f, 0.f};
#pragma unroll
            for (int k = 0; k < 8; ++k) sq = acc_sq(sq, v[c][k], f32x2{cm, cm});
            const float cq = wave_sum(sq.x + sq.y);
            if (lane == 0) stat[u][chunk_of(code, wave * 2 + c)] = f32x2{cm, cq};
        }
        __syncthreads();
        if (wave == 0) {
            unsigned ln = threadIdx.x;
            asm volatile("" : "+v"(ln));
            const f32x2 mine = stat[u][chunk_of(code, ln & 7)];
            const float mx = mine.x, my = mine.y;
            auto publish = [&](unsigned tag) {
                const unsigned hi = tag | code << 16 | mytag << 18;
                const u32x4 msg = {__builtin_bit_cast(unsigned, mx), hi, __builtin_bit_cast(unsigned, my), hi};
                if (ln < 8) __builtin_amdgcn_raw_buffer_store_b128(msg, rout, (u * 8 + ln) * 16u, 0, SC1);
            };
            if (verified) publish(ptag);
            bool posted = verified;
            const u64 t0 = __builtin_amdgcn_s_memrealtime();
            const unsigned entry = ln < 8 ? u * 8 + ln : ADVERT;
            u32x4 r;
            bool good;
            for (;;) {
                r = __builtin_amdgcn_raw_buffer_load_b128(rin, entry * 16u, 0, SC1);
                good = ln < 8 && (r[1] & 0xffffu) == mytag && r[3] == r[1];
                const bool all = (__ballot(good) & 0xffu) == 0xffu;
                if (!verified) {
                    const unsigned cand = __builtin_amdgcn_readlane(r[1], all ? 0 : 8) >> 18 & 0x3fu;
                    if (!posted || cand != ptag) publish(cand);
                    ptag = cand;
                    posted = true;
                    verified = all;
                }
                if (all || __builtin_amdgcn_s_memrealtime() - t0 > TIMEOUT_TICKS) break;
                __builtin_amdgcn_s_sleep(1);
            }
            unsigned have = set_of(code);
            const unsigned cp = chunk_of(r[1] >> 16 & 3u, ln & 7);
            const bool take = good && !(have >> cp & 1u);
            const unsigned r0 = r[0], r2 = r[2];
            if (take) stat[u][cp] = f32x2{__builtin_bit_cast(float, r0), __builtin_bit_cast(float, r2)};
            const unsigned got = take ? 1u << cp : 0u;
#pragma unroll
            for (int l = 0; l < 8; ++l) have |= __builtin_amdgcn_readlane(got, l);
            if (eager && u == 0) {
                const unsigned c = read_hint(hint, t_start, role);
                if (ln == 0) sh_code[1] = c;
            }
            if (ln == 0) sh_missing[u] = ~have & 0xffffu;
        }
        __syncthreads();
        const unsigned help = __builtin_amdgcn_readfirstlane(sh_missing[u]);
        if (help) {
#pragma unroll 1
            for (unsigned hc = wave; hc < NCHUNK; hc += 4) {
                if (!((help >> hc) & 1)) continue;
                const auto hx = chunk_rsrc(x + pbase + hc * CHUNK_BYTES);
                f32x2 hs = {0.f, 0.f};
#pragma unroll 1
                for (int k = 0; k < 8; ++k) hs = acc_sum(hs, __builtin_amdgcn_raw_buffer_load_b128(hx, voff + k * 1024u, 0, 0));
                const float hm = wave_sum(hs.x + hs.y) * (1.0f / CHUNK_ELEMS);
                f32x2 hq = {0.f, 0.f};
#pragma unroll 1
                for (int k = 0; k < 8; ++k)
                    hq = acc_sq(hq, __builtin_amdgcn_raw_buffer_load_b128(hx, voff + k * 1024u, 0, 0), f32x2{hm, hm});
                const float hq1 = wave_sum(hq.x + hq.y);
                if (lane == 0) stat[u][hc] = f32x2{hm, hq1};
            }
            __syncthreads();
        }
        const f32x2 e = stat[u][lane & 15];
        const float mean = row_sum(e.x) * (1.0f / NCHUNK);
        const float dm = e.x - mean;
        const float m2 = row_sum(__builtin_fmaf(dm * dm, (float)CHUNK_ELEMS, e.y));
        const float sc = gamma / __builtin_sqrtf(m2 * (1.0f / HW) + 1e-5f);
        const f32x2 m_2 = {mean, mean}, sc2 = {sc, sc}, sh2 = {beta, beta};
#pragma unroll
        for (int c = 0; c < 2; ++c) {
            const auto ry = chunk_rsrc(y + pbase + chunk_of(code, wave * 2 + c) * CHUNK_BYTES);
            asm volatile("" : "+v"(voff));
#pragma unroll
            for (int k = 0; k < 8; ++k) asm volatile("" : "+v"(v[c][k]));
#pragma unroll
            for (int k = 0; k < 8; ++k)
                __builtin_amdgcn_raw_buffer_store_b128(normalize(v[c][k], m_2, sc2, sh2), ry, voff + k * 1024u, 0, STAUX);
        }
        if (help) {
#pragma unroll 1
            for (unsigned hc = wave; hc < NCHUNK; hc += 4) {
                if (!((help >> hc) & 1)) continue;
                const auto hx = chunk_rsrc(x + pbase + hc * CHUNK_BYTES);
                const auto hy = chunk_rsrc(y + pbase + hc * CHUNK_BYTES);
#pragma unroll 1
                for (int k = 0; k < 8; ++k)
                    __builtin_amdgcn_raw_buffer_store_b128(
                        normalize(__builtin_amdgcn_raw_buffer_load_b128(hx, voff + k * 1024u, 0, 0), m_2, sc2, sh2), hy,
                        voff + k * 1024u, 0, STAUX);
            }
        }
        if (eager && u == 0) code = __builtin_amdgcn_readfirstlane(sh_code[1]);
    }
}

__global__ __launch_bounds__(1024, 8) void instnorm_plane_kernel(const char* __restrict__ x, const uint16_t* __restrict__ g,
                                                                 const uint16_t* __restrict__ b, char* __restrict__ y,
                                                                 int C) {
    __shared__ f32x2 stat[NCHUNK];
    const unsigned inst = blockIdx.x;
    const unsigned wave = __builtin_amdgcn_readfirstlane(threadIdx.x >> 6), lane = threadIdx.x & 63;
    const size_t off = (size_t)inst * PLANE_BYTES + wave * CHUNK_BYTES;
    const auto rx = chunk_rsrc(x + off);
    const auto ry = chunk_rsrc(y + off);
    u32x4 v[8];
#pragma unroll
    for (int k = 0; k < 8; ++k) v[k] = __builtin_amdgcn_raw_buffer_load_b128(rx, lane * 16u + k * 1024u, 0, LDAUX);
    const unsigned ch = inst % (unsigned)C;
    const float gamma = __builtin_bit_cast(float, (unsigned)g[ch] << 16);
    const float beta = __builtin_bit_cast(float, (unsigned)b[ch] << 16);
    f32x2 su = {0.f, 0.f};
#pragma unroll
    for (int k = 0; k < 8; ++k) su = acc_sum(su, v[k]);
    const float cm = wave_sum(su.x + su.y) * (1.0f / CHUNK_ELEMS);
#pragma unroll
    for (int k = 0; k < 8; ++k) asm volatile("" : "+v"(v[k]));
    f32x2 sq = {0.f, 0.f};
#pragma unroll
    for (int k = 0; k < 8; ++k) sq = acc_sq(sq, v[k], f32x2{cm, cm});
    const float cq = wave_sum(sq.x + sq.y);
    if (lane == 0) stat[wave] = f32x2{cm, cq};
    __syncthreads();
    const f32x2 e = stat[lane & 15];
    const float mean = row_sum(e.x) * (1.0f / NCHUNK);
    const float dm = e.x - mean;
    const float m2 = row_sum(__builtin_fmaf(dm * dm, (float)CHUNK_ELEMS, e.y));
    const float sc = gamma / __builtin_sqrtf(m2 * (1.0f / HW) + 1e-5f);
#pragma unroll
    for (int k = 0; k < 8; ++k) asm volatile("" : "+v"(v[k]));
#pragma unroll
    for (int k = 0; k < 8; ++k)
        __builtin_amdgcn_raw_buffer_store_b128(normalize(v[k], f32x2{mean, mean}, f32x2{sc, sc}, f32x2{beta, beta}), ry,
                                               lane * 16u + k * 1024u, 0, STAUX);
}

extern "C" void kernel_launch(void* const* d_in, const int* in_sizes, int n_in,
                              void* d_out, int out_size, void* d_ws, size_t ws_size,
                              hipStream_t stream) {
    const char* x = (const char*)d_in[0];
    const uint16_t* g = (const uint16_t*)d_in[1];
    const uint16_t* b = (const uint16_t*)d_in[2];
    const int C = in_sizes[1];
    const int n_inst = in_sizes[0] / HW;
    const unsigned nblocks = 2 * NPAGES * PLANES_PER_PAGE / UPW;
    if (n_inst == (int)(NPAGES * PLANES_PER_PAGE) && ws_size >= NPAGES * HINT_BYTES + (size_t)nblocks * BOX_BYTES)
        instnorm_pair_kernel<<<nblocks, 256, 0, stream>>>(x, g, b, (char*)d_out, (char*)d_ws, C);
    else
        instnorm_plane_kernel<<<n_inst, 1024, 0, stream>>>(x, g, b, (char*)d_out, C);
}
```

```cpp
#include <hip/hip_runtime.h>
#include <hip/hip_bf16.h>
#include <stdint.h>

typedef unsigned int u32x4 __attribute__((ext_vector_type(4)));
typedef float f32x2 __attribute__((ext_vector_type(2)));
typedef __bf16 bf16x2 __attribute__((ext_vector_type(2)));
typedef unsigned long long u64;
typedef __attribute__((address_space(1))) u64 gu64;

constexpr int HW = 2048 * 32;
constexpr unsigned PLANE_BYTES = HW * 2;
constexpr unsigned CHUNK_BYTES = 8192;
constexpr int NCHUNK = 16;
constexpr int PLANES_PER_PAGE = 16;
constexpr size_t PAGE_BYTES = (size_t)PLANES_PER_PAGE * PLANE_BYTES;
constexpr unsigned NPAGES = 80;
constexpr int UPW = 2;
constexpr unsigned BOX_BYTES = 384;
constexpr unsigned ADVERT = UPW * 8;
constexpr unsigned HINT_BYTES = 128;
constexpr int LDAUX = 2;
constexpr int STAUX = 16;
constexpr int SC1 = 16;
constexpr u64 TIMEOUT_TICKS = 5000;
constexpr unsigned HINT_WAIT = 2500;
constexpr unsigned HOLD = 80, AFTER = 10;
constexpr unsigned QUIET = 190;
constexpr unsigned EAGER = 16;
constexpr int NSAMP = 4;
constexpr unsigned CLEAR = 128, MARGIN = 64;

__device__ __forceinline__ float bf_lo(unsigned v) { return __builtin_bit_cast(float, v << 16); }
__device__ __forceinline__ float bf_hi(unsigned v) { return __builtin_bit_cast(float, v & 0xffff0000u); }
__device__ __forceinline__ unsigned pack_bf16(f32x2 f) {
    return __builtin_bit_cast(unsigned, __builtin_convertvector(f, bf16x2));
}
__device__ __forceinline__ unsigned xcc_id() {
    unsigned v;
    asm volatile("s_getreg_b32 %0, hwreg(HW_REG_XCC_ID)" : "=s"(v));
    return v & 0xf;
}
template <int CTRL> __device__ __forceinline__ float dpp_add(float v) {
    int o = __builtin_amdgcn_update_dpp(0, __builtin_bit_cast(int, v), CTRL, 0xf, 0xf, true);
    return v + __builtin_bit_cast(float, o);
}
__device__ __forceinline__ float row_sum(float v) {
    v = dpp_add<0xB1>(v);
    v = dpp_add<0x4E>(v);
    v = dpp_add<0x141>(v);
    v = dpp_add<0x140>(v);
    return v;
}
__device__ __forceinline__ float rl(float v, int l) {
    return __builtin_bit_cast(float, __builtin_amdgcn_readlane(__builtin_bit_cast(int, v), l));
}
__device__ __forceinline__ float wave_sum(float v) {
    v = row_sum(v);
    return (rl(v, 0) + rl(v, 16)) + (rl(v, 32) + rl(v, 48));
}
__device__ __forceinline__ f32x2 acc_sum(f32x2 s, u32x4 v) {
#pragma unroll
    for (int j = 0; j < 4; ++j)
        s = __builtin_elementwise_fma(f32x2{bf_lo(v[j]), bf_hi(v[j])}, f32x2{0x1p-12f, 0x1p-12f}, s);
    return s;
}
__device__ __forceinline__ f32x2 acc_sq(f32x2 q, u32x4 v, float m) {
    const f32x2 nm = {m * -0x1p-6f, m * -0x1p-6f};
#pragma unroll
    for (int j = 0; j < 4; ++j) {
        const f32x2 d = __builtin_elementwise_fma(f32x2{bf_lo(v[j]), bf_hi(v[j])}, f32x2{0x1p-6f, 0x1p-6f}, nm);
        q = __builtin_elementwise_fma(d, d, q);
    }
    return q;
}
__device__ __forceinline__ f32x2 combine(f32x2 e) {
    const float mean = row_sum(e.x * 0x1p-4f);
    const float dm = (e.x - mean) * 0x1p-2f;
    return f32x2{mean, row_sum(__builtin_fmaf(dm, dm, e.y * 0x1p-4f))};
}
__device__ __forceinline__ u32x4 normalize(u32x4 v, f32x2 m, f32x2 sc, f32x2 sh) {
    u32x4 o;
#pragma unroll
    for (int j = 0; j < 4; ++j) {
        f32x2 d = f32x2{bf_lo(v[j]), bf_hi(v[j])} - m;
        o[j] = pack_bf16(__builtin_elementwise_fma(d, sc, sh));
    }
    return o;
}
__device__ __forceinline__ unsigned hash32(unsigned v) {
    v *= 2654435761u; v ^= v >> 15; v *= 2246822519u; v ^= v >> 13;
    return v;
}
__device__ __forceinline__ unsigned umin_(unsigned a, unsigned b) { return a < b ? a : b; }
__device__ __forceinline__ auto chunk_rsrc(const char* p) {
    return __builtin_amdgcn_make_buffer_rsrc((void*)p, 0, CHUNK_BYTES, 0x00020000);
}
__device__ __forceinline__ unsigned probe_par(unsigned i) { return (i ^ (i >> 1)) & 1; }
__device__ __forceinline__ unsigned probe_off(unsigned pg, unsigned wave, unsigned i, unsigned salt) {
    const unsigned h = hash32(((pg * 4 + wave) * 16 + i) ^ salt * 0x9e3779b1u);
    return ((h >> 8) & 127) * (2 * CHUNK_BYTES) + probe_par(i) * CHUNK_BYTES + (h & 63) * 128;
}
__device__ __forceinline__ unsigned chunk_of(unsigned code, unsigned w) {
    return (code & 2) ? 2 * w + (code & 1) : (w >> 1) * 4 + (w & 1) + 2 * (code & 1);
}
__device__ __forceinline__ unsigned set_of(unsigned code) {
    return (code & 2) ? 0x5555u << (code & 1) : 0x3333u << (2 * (code & 1));
}
__device__ __forceinline__ unsigned read_hint(gu64* hint, u64 t_start, unsigned role) {
    const u64 lo = t_start - 2000;
    for (;;) {
        const u64 h = __hip_atomic_load(hint, __ATOMIC_RELAXED, __HIP_MEMORY_SCOPE_AGENT);
        const unsigned hl = __builtin_amdgcn_readfirstlane((unsigned)h);
        const unsigned hh = __builtin_amdgcn_readfirstlane((unsigned)(h >> 32));
        const u64 now = __builtin_amdgcn_s_memrealtime();
        const u64 age = ((((u64)hh << 32 | hl) >> 8) - lo) & 0xffffffffffffffull;
        if (age <= now - t_start + 2016) return (hl & 1u) ? 2u | (((hl >> 1) ^ (xcc_id() >> 2)) & 1u) : role;
        if (now - t_start > HINT_WAIT) return role;
        __builtin_amdgcn_s_sleep(2);
    }
}

__global__ __launch_bounds__(256, 5) void instnorm_pair_kernel(const char* __restrict__ x, const uint16_t* __restrict__ g,
                                                               const uint16_t* __restrict__ b, char* __restrict__ y,
                                                               char* ws, int C) {
    __shared__ f32x2 stat[UPW][NCHUNK];
    __shared__ unsigned sh_missing[UPW];
    __shared__ unsigned sh_code[UPW];
    __shared__ unsigned sh_lat[8];
    const u64 t_start = __builtin_amdgcn_s_memrealtime();
    const unsigned bid = blockIdx.x;
    const unsigned xcc = xcc_id();
    const unsigned role = (bid >> 2) & 1, q = bid >> 3;
    const unsigned grp = (bid & 3) ^ (role & ~(xcc - bid) & 1u);
    const unsigned box = q << 3 | role << 2 | grp;
    const unsigned wave = __builtin_amdgcn_readfirstlane(threadIdx.x >> 6), lane = threadIdx.x & 63;
    const unsigned sp = q >> 3, s5 = sp / 5, r5 = sp - s5 * 5;
    const unsigned page = (r5 == 4 ? NPAGES / 5 + s5 : s5 * 4 + r5) * 4 + grp;
    const unsigned inst0 = page * PLANES_PER_PAGE + (q & 7) * UPW;
    char* boxes = ws + NPAGES * HINT_BYTES;
    const auto rin = __builtin_amdgcn_make_buffer_rsrc(boxes + (size_t)box * BOX_BYTES, 0, BOX_BYTES, 0x00020000);
    char* outbox = boxes + (size_t)(box ^ 4) * BOX_BYTES;
    const auto rout = __builtin_amdgcn_make_buffer_rsrc(outbox, 0, BOX_BYTES, 0x00020000);
    gu64* hint = (gu64*)(ws + (size_t)page * HINT_BYTES);
    const bool eager = q < EAGER;

    unsigned code = role;
    {
        const bool quiet = xcc == 0 || xcc == 2;
        if (quiet && q < NPAGES / 2) {
            const unsigned pg = 2 * q + (xcc >> 1), salt = (unsigned)(t_start >> 11);
            const char* buf = wave < 2 ? x : y;
            {
                const char* t = lane < NSAMP ? buf + (size_t)(pg ^ 1) * PAGE_BYTES + probe_off(pg ^ 1, wave, lane, salt)
                                             : buf + (size_t)pg * PAGE_BYTES + (PAGE_BYTES - 128);
                unsigned w0;
                if (lane <= NSAMP)
                    asm volatile("global_load_dword %0, %1, off\n\ts_waitcnt vmcnt(0)" : "=v"(w0) : "v"(t) : "memory");
            }
            const u64 t1 = __builtin_amdgcn_s_memrealtime() + AFTER;
            while (__builtin_amdgcn_s_memrealtime() < t1 || __builtin_amdgcn_s_memrealtime() - t_start < HOLD)
                __builtin_amdgcn_s_sleep(1);
            unsigned lat[2] = {4095u, 4095u};
#pragma unroll
            for (int i = 0; i < NSAMP; ++i) {
                if (i == 2 && lat[0] < 4095u && lat[1] < 4095u && (lat[0] + CLEAR <= lat[1] || lat[1] + CLEAR <= lat[0]))
                    break;
                const char* p = buf + (size_t)pg * PAGE_BYTES + probe_off(pg, wave, i, salt);
                u64 c0, c1;
                unsigned v;
                if (lane == 0)
                    asm volatile("s_memtime %0\n\ts_waitcnt lgkmcnt(0)\n\t"
                                 "global_load_dword %2, %3, off\n\ts_waitcnt vmcnt(0)\n\t"
                                 "s_memtime %1\n\ts_waitcnt lgkmcnt(0)"
                                 : "=&s"(c0), "=&s"(c1), "=&v"(v) : "v"(p) : "memory");
                unsigned d = __builtin_amdgcn_readfirstlane((unsigned)(c1 - c0));
                d = d < 336u ? 4095u : d;
                lat[probe_par(i)] = umin_(lat[probe_par(i)], d);
            }
            if (lane == 0) {
                sh_lat[wave * 2] = lat[0];
                sh_lat[wave * 2 + 1] = lat[1];
            }
            __syncthreads();
            if (threadIdx.x == 0) {
                const unsigned xe = umin_(sh_lat[0], sh_lat[2]), xo = umin_(sh_lat[1], sh_lat[3]);
                const unsigned ye = umin_(sh_lat[4], sh_lat[6]), yo = umin_(sh_lat[5], sh_lat[7]);
                const unsigned ev = xe + MARGIN <= xo && ye + MARGIN <= yo, od = xo + MARGIN <= xe && yo + MARGIN <= ye;
                __hip_atomic_store((gu64*)(ws + (size_t)pg * HINT_BYTES),
                                   (u64)((ev | od) | od << 1) | __builtin_amdgcn_s_memrealtime() << 8, __ATOMIC_RELAXED,
                                   __HIP_MEMORY_SCOPE_AGENT);
            }
        }
        if (quiet) {
            while (__builtin_amdgcn_s_memrealtime() - t_start < (q < 48 ? QUIET + 40 : QUIET)) __builtin_amdgcn_s_sleep(2);
        } else if (!eager) {
            const char* t = (lane == 0 ? x : y) + (size_t)inst0 * PLANE_BYTES + wave * 4096;
            unsigned w0;
            if (lane < 2) asm volatile("global_load_dword %0, %1, off\n\ts_waitcnt vmcnt(0)" : "=v"(w0) : "v"(t) : "memory");
        }
        if (!eager) {
            if (wave == 0) {
                const unsigned c = read_hint(hint, t_start, role);
                if (lane == 0) sh_code[0] = c;
            }
            __syncthreads();
            code = __builtin_amdgcn_readfirstlane(sh_code[0]);
        }
    }

    unsigned mytag = 0, ptag = 0;
    bool verified = false;
#pragma unroll 1
    for (unsigned u = 0; u < UPW; ++u) {
        const unsigned inst = inst0 + u;
        const size_t pbase = (size_t)inst * PLANE_BYTES;
        unsigned voff = lane * 16u;
        asm volatile("" : "+v"(voff));
        const bool first = u == 0 && wave == 0;
        u32x4 old = {0, 0, 0, 0};
        if (first) old = __builtin_amdgcn_raw_buffer_load_b128(rin, voff, 0, SC1);
        u32x4 v[2][8];
#pragma unroll
        for (int c = 0; c < 2; ++c) {
            const auto rx = chunk_rsrc(x + pbase + chunk_of(code, wave * 2 + c) * CHUNK_BYTES);
#pragma unroll
            for (int k = 0; k < 8; ++k) v[c][k] = __builtin_amdgcn_raw_buffer_load_b128(rx, voff + k * 1024u, 0, LDAUX);
        }
        const unsigned ch = inst % (unsigned)C;
        const float gamma = __builtin_bit_cast(float, (unsigned)g[ch] << 16);
        const float beta = __builtin_bit_cast(float, (unsigned)b[ch] << 16);
        if (first) {
            const unsigned t0 = old[1] & 0x3fu, t1 = old[3] & 0x3fu;
            mytag = 1;
            while (__ballot(t0 == mytag || t1 == mytag)) ++mytag;
            if (lane == 0)
                __hip_atomic_store((gu64*)(outbox + ADVERT * 16), (u64)(mytag << 18) << 32, __ATOMIC_RELAXED,
                                   __HIP_MEMORY_SCOPE_AGENT);
        }
#pragma unroll
        for (int c = 0; c < 2; ++c) {
            f32x2 su = {0.f, 0.f};
#pragma unroll
            for (int k = 0; k < 8; ++k) su = acc_sum(su, v[c][k]);
            const float cm = wave_sum(su.x + su.y);
#pragma unroll
            for (int k = 0; k < 8; ++k) asm volatile("" : "+v"(v[c][k]));
            f32x2 sq = {0.f, 0.f};
#pragma unroll
            for (int k = 0; k < 8; ++k) sq = acc_sq(sq, v[c][k], cm);
            const float cq = wave_sum(sq.x + sq.y);
            if (lane == 0) stat[u][chunk_of(code, wave * 2 + c)] = f32x2{cm, cq};
        }
        __syncthreads();
        if (wave == 0) {
            unsigned ln = threadIdx.x;
            asm volatile("" : "+v"(ln));
            const f32x2 mine = stat[u][chunk_of(code, ln & 7)];
            const float mx = mine.x, my = mine.y;
            auto publish = [&](unsigned tag) {
                const unsigned hi = tag | code << 16 | mytag << 18;
                const u32x4 msg = {__builtin_bit_cast(unsigned, mx), hi, __builtin_bit_cast(unsigned, my), hi};
                if (ln < 8) __builtin_amdgcn_raw_buffer_store_b128(msg, rout, (u * 8 + ln) * 16u, 0, SC1);
            };
            if (verified) publish(ptag);
            bool posted = verified;
            const u64 t0 = __builtin_amdgcn_s_memrealtime();
            const unsigned entry = ln < 8 ? u * 8 + ln : ADVERT;
            u32x4 r;
            bool good;
            for (;;) {
                r = __builtin_amdgcn_raw_buffer_load_b128(rin, entry * 16u, 0, SC1);
                good = ln < 8 && (r[1] & 0xffffu) == mytag && r[3] == r[1];
                const bool all = (__ballot(good) & 0xffu) == 0xffu;
                if (!verified) {
                    const unsigned cand = __builtin_amdgcn_readlane(r[1], all ? 0 : 8) >> 18 & 0x3fu;
                    if (!posted || cand != ptag) publish(cand);
                    ptag = cand;
                    posted = true;
                    verified = all;
                }
                if (all || __builtin_amdgcn_s_memrealtime() - t0 > TIMEOUT_TICKS) break;
                __builtin_amdgcn_s_sleep(1);
            }
            unsigned have = set_of(code);
            const unsigned cp = chunk_of(r[1] >> 16 & 3u, ln & 7);
            const bool take = good && !(have >> cp & 1u);
            const unsigned r0 = r[0], r2 = r[2];
            if (take) stat[u][cp] = f32x2{__builtin_bit_cast(float, r0), __builtin_bit_cast(float, r2)};
            const unsigned got = take ? 1u << cp : 0u;
#pragma unroll
            for (int l = 0; l < 8; ++l) have |= __builtin_amdgcn_readlane(got, l);
            if (eager && u == 0) {
                const unsigned c = read_hint(hint, t_start, role);
                if (ln == 0) sh_code[1] = c;
            }
            if (ln == 0) sh_missing[u] = ~have & 0xffffu;
        }
        __syncthreads();
        const unsigned help = __builtin_amdgcn_readfirstlane(sh_missing[u]);
        if (help) {
#pragma unroll 1
            for (unsigned hc = wave; hc < NCHUNK; hc += 4) {
                if (!((help >> hc) & 1)) continue;
                const auto hx = chunk_rsrc(x + pbase + hc * CHUNK_BYTES);
                f32x2 hs = {0.f, 0.f};
#pragma unroll 1
                for (int k = 0; k < 8; ++k) hs = acc_sum(hs, __builtin_amdgcn_raw_buffer_load_b128(hx, voff + k * 1024u, 0, 0));
                const float hm = wave_sum(hs.x + hs.y);
                f32x2 hq = {0.f, 0.f};
#pragma unroll 1
                for (int k = 0; k < 8; ++k)
                    hq = acc_sq(hq, __builtin_amdgcn_raw_buffer_load_b128(hx, voff + k * 1024u, 0, 0), hm);
                const float hq1 = wave_sum(hq.x + hq.y);
                if (lane == 0) stat[u][hc] = f32x2{hm, hq1};
            }
            __syncthreads();
        }
        const f32x2 mv = combine(stat[u][lane & 15]);
        const float mean = mv.x;
        const float sc = gamma / __builtin_sqrtf(mv.y + 1e-5f);
        const f32x2 m_2 = {mean, mean}, sc2 = {sc, sc}, sh2 = {beta, beta};
#pragma unroll
        for (int c = 0; c < 2; ++c) {
            const auto ry = chunk_rsrc(y + pbase + chunk_of(code, wave * 2 + c) * CHUNK_BYTES);
            asm volatile("" : "+v"(voff));
#pragma unroll
            for (int k = 0; k < 8; ++k) asm volatile("" : "+v"(v[c][k]));
#pragma unroll
            for (int k = 0; k < 8; ++k)
                __builtin_amdgcn_raw_buffer_store_b128(normalize(v[c][k], m_2, sc2, sh2), ry, voff + k * 1024u, 0, STAUX);
        }
        if (help) {
#pragma unroll 1
            for (unsigned hc = wave; hc < NCHUNK; hc += 4) {
                if (!((help >> hc) & 1)) continue;
                const auto hx = chunk_rsrc(x + pbase + hc * CHUNK_BYTES);
                const auto hy = chunk_rsrc(y + pbase + hc * CHUNK_BYTES);
#pragma unroll 1
                for (int k = 0; k < 8; ++k)
                    __builtin_amdgcn_raw_buffer_store_b128(
                        normalize(__builtin_amdgcn_raw_buffer_load_b128(hx, voff + k * 1024u, 0, 0), m_2, sc2, sh2), hy,
                        voff + k * 1024u, 0, STAUX);
            }
        }
        if (eager && u == 0) code = __builtin_amdgcn_readfirstlane(sh_code[1]);
    }
}

__global__ __launch_bounds__(1024, 8) void instnorm_plane_kernel(const char* __restrict__ x, const uint16_t* __restrict__ g,
                                                                 const uint16_t* __restrict__ b, char* __restrict__ y,
                                                                 int C) {
    __shared__ f32x2 stat[NCHUNK];
    const unsigned inst = blockIdx.x;
    const unsigned wave = __builtin_amdgcn_readfirstlane(threadIdx.x >> 6), lane = threadIdx.x & 63;
    const size_t off = (size_t)inst * PLANE_BYTES + wave * CHUNK_BYTES;
    const auto rx = chunk_rsrc(x + off);
    const auto ry = chunk_rsrc(y + off);
    u32x4 v[8];
#pragma unroll
    for (int k = 0; k < 8; ++k) v[k] = __builtin_amdgcn_raw_buffer_load_b128(rx, lane * 16u + k * 1024u, 0, LDAUX);
    const unsigned ch = inst % (unsigned)C;
    const float gamma = __builtin_bit_cast(float, (unsigned)g[ch] << 16);
    const float beta = __builtin_bit_cast(float, (unsigned)b[ch] << 16);
    f32x2 su = {0.f, 0.f};
#pragma unroll
    for (int k = 0; k < 8; ++k) su = acc_sum(su, v[k]);
    const float cm = wave_sum(su.x + su.y);
#pragma unroll
    for (int k = 0; k < 8; ++k) asm volatile("" : "+v"(v[k]));
    f32x2 sq = {0.f, 0.f};
#pragma unroll
    for (int k = 0; k < 8; ++k) sq = acc_sq(sq, v[k], cm);
    const float cq = wave_sum(sq.x + sq.y);
    if (lane == 0) stat[wave] = f32x2{cm, cq};
    __syncthreads();
    const f32x2 mv = combine(stat[lane & 15]);
    const float mean = mv.x;
    const float sc = gamma / __builtin_sqrtf(mv.y + 1e-5f);
#pragma unroll
    for (int k = 0; k < 8; ++k) asm volatile("" : "+v"(v[k]));
#pragma unroll
    for (int k = 0; k < 8; ++k)
        __builtin_amdgcn_raw_buffer_store_b128(normalize(v[k], f32x2{mean, mean}, f32x2{sc, sc}, f32x2{beta, beta}), ry,
                                               lane * 16u + k * 1024u, 0, STAUX);
}

extern "C" void kernel_launch(void* const* d_in, const int* in_sizes, int n_in,
                              void* d_out, int out_size, void* d_ws, size_t ws_size,
                              hipStream_t stream) {
    const char* x = (const char*)d_in[0];
    const uint16_t* g = (const uint16_t*)d_in[1];
    const uint16_t* b = (const uint16_t*)d_in[2];
    const int C = in_sizes[1];
    const int n_inst = in_sizes[0] / HW;
    const unsigned nblocks = 2 * NPAGES * PLANES_PER_PAGE / UPW;
    if (n_inst == (int)(NPAGES * PLANES_PER_PAGE) && ws_size >= NPAGES * HINT_BYTES + (size_t)nblocks * BOX_BYTES)
        instnorm_pair_kernel<<<nblocks, 256, 0, stream>>>(x, g, b, (char*)d_out, (char*)d_ws, C);
    else
        instnorm_plane_kernel<<<n_inst, 1024, 0, stream>>>(x, g, b, (char*)d_out, C);
}
```
